```python
import torch

def _softmax(x, axis=-1):
    return torch.softmax(x, dim=axis)
def _gelu(x):
    return torch.nn.functional.gelu(x, approximate="tanh")
def _silu(x):
    return torch.nn.functional.silu(x)
def _sigmoid(x):
    return torch.sigmoid(x)
def _layernorm(x, axis=-1, eps=1e-5):
    m = x.mean(dim=axis, keepdim=True)
    v = x.var(dim=axis, keepdim=True, unbiased=False)
    return (x - m) / torch.sqrt(v + eps)
def _scatter_add(tgt, idx, src, axis=0):
    return tgt.clone().index_add_(axis, idx, src)
def _segment_sum(src, seg, n):
    out = torch.zeros((n,) + src.shape[1:], dtype=src.dtype)
    return out.index_add_(0, seg, src)
def _pad(x, pw):
    flat = tuple(v for pair in reversed(pw) for v in pair)
    return torch.nn.functional.pad(x, flat)

kernel_name = 'concat_heads__mha_block_cached__gqa_block_cached__near_ridge'

def setup_inputs():

    _w = (lambda a: (lambda t: t.to(torch.bfloat16) if t.is_floating_point() else t)(torch.as_tensor(a)))
    def _A_setup():
        B, Sq, D = 8, 192, 512
        H, Sk, Dh = 32, 1024, 128
        return {
            "x": _w((lambda *s: torch.randn(*s, dtype=torch.bfloat16) * 0.8)(B, Sq, D)),
            "k_cache": _w((lambda *s: torch.randn(*s, dtype=torch.bfloat16) * 0.8)(B, H, Sk, Dh)),
            "v_cache": _w((lambda *s: torch.randn(*s, dtype=torch.bfloat16) * 0.05)(B, H, Sk, Dh)),
            "wq": _w((lambda *s: torch.randn(*s, dtype=torch.bfloat16) * 0.8)(D, H * Dh) * (D ** -0.5)),
            "wo": _w((lambda *s: torch.randn(*s, dtype=torch.bfloat16) * 0.05)(H * Dh, D) * ((H * Dh) ** -0.5)),
        }
    _A_in = _A_setup()

    def _B_setup():
        B, Sq, D = 8, 192, 512
        Hq, Hkv, Sk, Dh = 32, 32, 1024, 128
        assert Hq % Hkv == 0, (Hq, Hkv)
        return {
            "x": _w((lambda *s: torch.randn(*s, dtype=torch.bfloat16) * 0.8)(B, Sq, D)),
            "k_cache": _w((lambda *s: torch.randn(*s, dtype=torch.bfloat16) * 0.8)(B, Hkv, Sk, Dh)),
            "v_cache": _w((lambda *s: torch.randn(*s, dtype=torch.bfloat16) * 0.05)(B, Hkv, Sk, Dh)),
            "wq": _w((lambda *s: torch.randn(*s, dtype=torch.bfloat16) * 0.8)(D, Hq * Dh) * (D ** -0.5)),
            "wo": _w((lambda *s: torch.randn(*s, dtype=torch.bfloat16) * 0.05)(Hq * Dh, D) * ((Hq * Dh) ** -0.5)),
        }
    _B_in = _B_setup()

    return {
        "A_x": _A_in["x"],
        "A_k_cache": _A_in["k_cache"],
        "A_v_cache": _A_in["v_cache"],
        "A_wq": _A_in["wq"],
        "A_wo": _A_in["wo"],
        "B_x": _B_in["x"],
        "B_k_cache": _B_in["k_cache"],
        "B_v_cache": _B_in["v_cache"],
        "B_wq": _B_in["wq"],
        "B_wo": _B_in["wo"],
    }

def reference(A_x, A_k_cache, A_v_cache, A_wq, A_wo, B_x, B_k_cache, B_v_cache, B_wq, B_wo):
    def _A_ref(x, k_cache, v_cache, wq, wo):
        B, Sq, D = x.shape
        H, Dh = 32, 128
        q = (lambda x, *d: x.permute(*d))((x @ wq).reshape(B, Sq, H, Dh), 0, 2, 1, 3)
        scores = torch.einsum("bhqd,bhkd->bhqk", q, k_cache) * (Dh ** -0.5)
        p = _softmax(scores, axis=-1)
        o = torch.einsum("bhqk,bhkd->bhqd", p, v_cache)
        return (lambda x, *d: x.permute(*d))(o, 0, 2, 1, 3).reshape(B, Sq, H * Dh) @ wo

    def _B_ref(x, k_cache, v_cache, wq, wo):
        B, Sq, D = x.shape
        Hq, Dh = 32, 128
        Hkv = k_cache.shape[1]
        rep = Hq // Hkv
        q = (lambda x, *d: x.permute(*d))((x @ wq).reshape(B, Sq, Hq, Dh), 0, 2, 1, 3)
        k = (lambda x, n, axis: torch.repeat_interleave(x, n, dim=axis))(k_cache, rep, axis=1)
        v = (lambda x, n, axis: torch.repeat_interleave(x, n, dim=axis))(v_cache, rep, axis=1)
        scores = torch.einsum("bhqd,bhkd->bhqk", q, k) * (Dh ** -0.5)
        p = _softmax(scores, axis=-1)
        o = torch.einsum("bhqk,bhkd->bhqd", p, v)
        return (lambda x, *d: x.permute(*d))(o, 0, 2, 1, 3).reshape(B, Sq, Hq * Dh) @ wo

    a = _A_ref(A_x, A_k_cache, A_v_cache, A_wq, A_wo)
    b = _B_ref(B_x, B_k_cache, B_v_cache, B_wq, B_wo)
    return torch.concatenate([a, b], axis=-1)
```

```cpp
#include <hip/hip_runtime.h>
#include <hip/hip_bf16.h>

static constexpr int NB = 8, SQ = 192, DM = 512, NH = 32, SK = 1024, DH = 128;
static constexpr int HD = NH * DH;

typedef __attribute__((ext_vector_type(8))) short bf16x8;
typedef __attribute__((ext_vector_type(4))) short s16x4;
typedef __attribute__((ext_vector_type(4))) float f32x4;
typedef __attribute__((address_space(3))) s16x4 lds_s16x4;
typedef unsigned short u16;

__device__ __forceinline__ unsigned cvt_pk(float lo, float hi) {
  typedef __attribute__((ext_vector_type(2))) __bf16 bf2_t;
  typedef __attribute__((ext_vector_type(2))) float f2_t;
  bf2_t v = __builtin_convertvector((f2_t){lo, hi}, bf2_t);
  return *reinterpret_cast<unsigned*>(&v);
}
__device__ __forceinline__ bf16x8 pack8(const f32x4& a, const f32x4& b) {
  typedef __attribute__((ext_vector_type(4))) unsigned u32x4;
  u32x4 w = {cvt_pk(a[0], a[1]), cvt_pk(a[2], a[3]), cvt_pk(b[0], b[1]), cvt_pk(b[2], b[3])};
  return *reinterpret_cast<bf16x8*>(&w);
}
__device__ __forceinline__ s16x4 tr_read(const char* lds_base, int byte_off) {
  return __builtin_amdgcn_ds_read_tr16_b64_v4i16((lds_s16x4*)(lds_base + byte_off));
}
__device__ __forceinline__ bf16x8 join(s16x4 a, s16x4 b) {
  return (bf16x8){a[0], a[1], a[2], a[3], b[0], b[1], b[2], b[3]};
}
__device__ __forceinline__ float xmax16_32(float v) {
  auto r = __builtin_amdgcn_permlane32_swap(__float_as_uint(v), __float_as_uint(v), false, false);
  v = fmaxf(__uint_as_float(r[0]), __uint_as_float(r[1]));
  auto s = __builtin_amdgcn_permlane16_swap(__float_as_uint(v), __float_as_uint(v), false, false);
  return fmaxf(__uint_as_float(s[0]), __uint_as_float(s[1]));
}
__device__ __forceinline__ float xsum16_32(float v) {
  auto r = __builtin_amdgcn_permlane32_swap(__float_as_uint(v), __float_as_uint(v), false, false);
  v = __uint_as_float(r[0]) + __uint_as_float(r[1]);
  auto s = __builtin_amdgcn_permlane16_swap(__float_as_uint(v), __float_as_uint(v), false, false);
  return __uint_as_float(s[0]) + __uint_as_float(s[1]);
}
__device__ __forceinline__ void glds16(const void* gsrc, unsigned lds_dst) {
  unsigned keep;
  asm volatile("s_mov_b32 %0, m0\n\ts_mov_b32 m0, %2\n\ts_nop 0\n\tglobal_load_lds_dwordx4 %1, off\n\ts_mov_b32 m0, %0" : "=&s"(keep) : "v"(gsrc), "s"(lds_dst) : "memory");
}
#define GLDS16(src, dst) __builtin_amdgcn_global_load_lds((const unsigned*)(src), (unsigned*)(dst), 16, 0, 0)

__global__ __launch_bounds__(256, 2)
void attn_kernel(const u16* __restrict__ xA, const u16* __restrict__ kA, const u16* __restrict__ vA, const u16* __restrict__ wqA,
                 const u16* __restrict__ xB, const u16* __restrict__ kB, const u16* __restrict__ vB, const u16* __restrict__ wqB,
                 u16* __restrict__ ows) {
  __shared__ __attribute__((aligned(16))) char lds[65536];
  const int bid = blockIdx.x, blk = bid >> 8, b = (bid >> 5) & 7, h = bid & 31;
  const u16* x  = blk ? xB : xA;
  const u16* kc = (blk ? kB : kA) + (size_t)(b * NH + h) * SK * DH;
  const u16* vc = (blk ? vB : vA) + (size_t)(b * NH + h) * SK * DH;
  const u16* wq = blk ? wqB : wqA;
  const int tid = threadIdx.x, w = tid >> 6, lane = tid & 63, g = lane >> 4, r16 = lane & 15;
  const int q4 = r16 >> 2, p4 = lane & 3;

  f32x4 qacc[8][3];
#pragma unroll
  for (int e = 0; e < 8; ++e)
#pragma unroll
    for (int qb = 0; qb < 3; ++qb) qacc[e][qb] = (f32x4){0.f, 0.f, 0.f, 0.f};
  const u16* xw = x + (size_t)(b * SQ + w * 48 + r16) * DM + 8 * g;
  const unsigned lds0 = (unsigned)(size_t)lds;
  {
    auto issue_w = [&](int c) {
      const unsigned slot = lds0 + (c & 3) * 16384 + 16 * w * 256;
#pragma unroll
      for (int i = 0; i < 4; ++i) {
        const int row = 16 * w + 4 * i + g;
        glds16(wq + (size_t)(c * 64 + row) * HD + h * DH + ((r16 ^ ((row & 3) << 2)) * 8), __builtin_amdgcn_readfirstlane(slot + i * 1024));
      }
    };
    bf16x8 xf[2][2][3];
#pragma unroll
    for (int t = 0; t < 2; ++t)
#pragma unroll
      for (int qb = 0; qb < 3; ++qb) xf[0][t][qb] = *(const bf16x8*)(xw + (size_t)qb * 16 * DM + 32 * t);
    issue_w(0); issue_w(1); issue_w(2);
#pragma unroll
    for (int c = 0; c < 8; ++c) {
      if (c + 1 < 8) {
#pragma unroll
        for (int t = 0; t < 2; ++t)
#pragma unroll
          for (int qb = 0; qb < 3; ++qb) xf[(c + 1) & 1][t][qb] = *(const bf16x8*)(xw + (size_t)qb * 16 * DM + (c + 1) * 64 + 32 * t);
      }
      if (c + 2 < 8) asm volatile("s_waitcnt vmcnt(10)" ::: "memory");
      else if (c + 1 < 8) asm volatile("s_waitcnt vmcnt(6)" ::: "memory");
      else asm volatile("s_waitcnt vmcnt(0)" ::: "memory");
      __builtin_amdgcn_s_barrier();
      asm volatile("" ::: "memory");
      if (c + 3 < 8) issue_w(c + 3);
      const char* wl = lds + (c & 3) * 16384;
#pragma unroll
      for (int t = 0; t < 2; ++t) {
#pragma unroll
        for (int e = 0; e < 8; ++e) {
          const int s = e >> 1, bb = e & 1;
          const int row0 = 32 * t + 8 * g + q4;
          const int chs = ((4 * s + p4) ^ (q4 << 2)) * 16 + 8 * bb;
          s16x4 a0 = tr_read(wl, row0 * 256 + chs);
          s16x4 a1 = tr_read(wl, (row0 + 4) * 256 + chs);
          bf16x8 a = join(a0, a1);
#pragma unroll
          for (int qb = 0; qb < 3; ++qb) qacc[e][qb] = __builtin_amdgcn_mfma_f32_16x16x32_bf16(a, xf[c & 1][t][qb], qacc[e][qb], 0, 0, 0);
        }
      }
    }
    __builtin_amdgcn_s_barrier();
    asm volatile("" ::: "memory");
  }
  bf16x8 qf[3][4];
#pragma unroll
  for (int qb = 0; qb < 3; ++qb)
#pragma unroll
    for (int s = 0; s < 4; ++s) qf[qb][s] = pack8(qacc[2 * s][qb], qacc[2 * s + 1][qb]);

  constexpr int NT = SK / 32;
  auto issue = [&](int j) {
    const u16* kg = kc + (size_t)j * 32 * DH;
    const u16* vg = vc + (size_t)j * 32 * DH;
    const unsigned slot = lds0 + (j & 3) * 16384 + 8 * w * 256;
#pragma unroll
    for (int i = 0; i < 2; ++i) {
      const int row = 8 * w + 4 * i + g;
      glds16(kg + row * DH + ((r16 ^ (row & 15)) * 8), __builtin_amdgcn_readfirstlane(slot + i * 1024));
      glds16(vg + row * DH + ((r16 ^ ((row & 7) << 1)) * 8), __builtin_amdgcn_readfirstlane(slot + 8192 + i * 1024));
    }
  };
  const float C = 0.08838834764831845f * 1.4426950408889634f;
  f32x4 oacc[8][3];
#pragma unroll
  for (int db = 0; db < 8; ++db)
#pragma unroll
    for (int qb = 0; qb < 3; ++qb) oacc[db][qb] = (f32x4){0.f, 0.f, 0.f, 0.f};
  float mrun[3] = {-1e30f, -1e30f, -1e30f}, lrun[3] = {0.f, 0.f, 0.f};

  auto qk = [&](int j, f32x4 (&sc)[2][3]) {
    const char* kl = lds + (j & 3) * 16384;
#pragma unroll
    for (int kb = 0; kb < 2; ++kb) {
#pragma unroll
      for (int qb = 0; qb < 3; ++qb) sc[kb][qb] = (f32x4){0.f, 0.f, 0.f, 0.f};
#pragma unroll
      for (int s = 0; s < 4; ++s) {
        bf16x8 kf = *(const bf16x8*)(kl + (16 * kb + r16) * 256 + (((4 * s + g) ^ r16) * 16));
#pragma unroll
        for (int qb = 0; qb < 3; ++qb) sc[kb][qb] = __builtin_amdgcn_mfma_f32_16x16x32_bf16(kf, qf[qb][s], sc[kb][qb], 0, 0, 0);
      }
    }
  };
  auto body = [&](int j) {
    if (j + 2 < NT) asm volatile("s_waitcnt vmcnt(8)" ::: "memory");
    else if (j + 1 < NT) asm volatile("s_waitcnt vmcnt(4)" ::: "memory");
    else asm volatile("s_waitcnt vmcnt(0)" ::: "memory");
    __builtin_amdgcn_s_barrier();
    asm volatile("" ::: "memory");
    if (j + 3 < NT) issue(j + 3);
    f32x4 cur[2][3];
    qk(j, cur);
    float mxq[3];
    bool need = false;
    const float THR = 8.0f / 0.08838834764831845f;
#pragma unroll
    for (int qb = 0; qb < 3; ++qb) {
      float mx = cur[0][qb][0];
#pragma unroll
      for (int kb = 0; kb < 2; ++kb)
#pragma unroll
        for (int i = 0; i < 4; ++i) mx = fmaxf(mx, cur[kb][qb][i]);
      mxq[qb] = xmax16_32(mx);
      need |= (mxq[qb] > mrun[qb] + THR);
    }
    if (__any(need)) {
#pragma unroll
      for (int qb = 0; qb < 3; ++qb) {
        const float mnew = fmaxf(mrun[qb], mxq[qb]);
        const float alpha = __builtin_amdgcn_exp2f((mrun[qb] - mnew) * C);
        mrun[qb] = mnew;
        lrun[qb] *= alpha;
#pragma unroll
        for (int db = 0; db < 8; ++db) oacc[db][qb] *= alpha;
      }
    }
#pragma unroll
    for (int qb = 0; qb < 3; ++qb) {
      const float mc = -mrun[qb] * C;
      float ps = 0.f;
#pragma unroll
      for (int kb = 0; kb < 2; ++kb)
#pragma unroll
        for (int i = 0; i < 4; ++i) { float p = __builtin_amdgcn_exp2f(fmaf(cur[kb][qb][i], C, mc)); cur[kb][qb][i] = p; ps += p; }
      lrun[qb] += ps;
    }
    bf16x8 pf[3];
#pragma unroll
    for (int qb = 0; qb < 3; ++qb) pf[qb] = pack8(cur[0][qb], cur[1][qb]);
    const char* vl = lds + (j & 3) * 16384 + 8192;
#pragma unroll
    for (int db = 0; db < 8; ++db) {
      const int row0 = 4 * g + q4;
      const int c0 = (((2 * db + (p4 >> 1)) ^ ((row0 & 7) << 1)) * 16) + 8 * (p4 & 1);
      s16x4 a0 = tr_read(vl, row0 * 256 + c0);
      s16x4 a1 = tr_read(vl, (row0 + 16) * 256 + c0);
      bf16x8 a = join(a0, a1);
#pragma unroll
      for (int qb = 0; qb < 3; ++qb) oacc[db][qb] = __builtin_amdgcn_mfma_f32_16x16x32_bf16(a, pf[qb], oacc[db][qb], 0, 0, 0);
    }
  };
  issue(0); issue(1); issue(2);
  for (int j = 0; j < NT; ++j) body(j);
  __syncthreads();
  char* ol = lds + w * 12288;
#pragma unroll
  for (int qb = 0; qb < 3; ++qb) {
    const float inv = 1.0f / xsum16_32(lrun[qb]);
    const int row = qb * 16 + r16;
#pragma unroll
    for (int db = 0; db < 8; ++db) {
      f32x4 v = oacc[db][qb] * inv;
      uint2 pk = {cvt_pk(v[0], v[1]), cvt_pk(v[2], v[3])};
      *(uint2*)(ol + row * 256 + (((2 * db + (g >> 1)) ^ r16) * 16) + 8 * (g & 1)) = pk;
    }
  }
  __syncthreads();
  u16* obase = ows + ((size_t)blk * (NB * SQ) + b * SQ + w * 48) * HD + h * DH;
#pragma unroll
  for (int i = 0; i < 12; ++i) {
    const int row = 4 * i + g;
    uint4 v = *(const uint4*)(ol + row * 256 + ((r16 ^ (row & 15)) * 16));
    *(uint4*)(obase + (size_t)row * HD + r16 * 8) = v;
  }
}

__global__ __launch_bounds__(512, 1)
void oproj_kernel(const u16* __restrict__ ows, const u16* __restrict__ woA, const u16* __restrict__ woB, u16* __restrict__ out) {
  __shared__ __attribute__((aligned(16))) char lds[3 * 40960];
  const int bid = blockIdx.x, combo = (bid & 7) * 4 + (bid >> 6), blk = combo >> 4, tm = combo & 15, tn = (bid >> 3) & 7;
  const u16* A = ows + ((size_t)blk * (NB * SQ) + tm * 96) * HD;
  const u16* W = (blk ? woB : woA) + tn * 64;
  const int tid = threadIdx.x, w = tid >> 6, lane = tid & 63, g = lane >> 4, r16 = lane & 15, q4 = r16 >> 2, p4 = lane & 3;
  const int kh = w >> 2, wm = (w >> 1) & 1, wn = w & 1;
  const unsigned lds0 = (unsigned)(size_t)lds;
  constexpr int NS = HD / 128;
  auto issue = [&](int kt) {
    const unsigned base = lds0 + (kt % 3) * 40960;
#pragma unroll
    for (int i = 0; i < 3; ++i) {
      const int r0 = (w * 3 + i) * 4, row = r0 + g;
      glds16(A + (size_t)row * HD + kt * 128 + ((r16 ^ (row & 15)) * 8), __builtin_amdgcn_readfirstlane(base + r0 * 256));
    }
#pragma unroll
    for (int i = 0; i < 2; ++i) {
      const int r0 = (w * 2 + i) * 8, row = r0 + (lane >> 3), c = lane & 7;
      glds16(W + (size_t)(kt * 128 + row) * DM + ((c ^ (row & 7)) * 8), __builtin_amdgcn_readfirstlane(base + 24576 + r0 * 128));
    }
  };
  f32x4 acc[3][2];
#pragma unroll
  for (int i = 0; i < 3; ++i) { acc[i][0] = (f32x4){0.f, 0.f, 0.f, 0.f}; acc[i][1] = (f32x4){0.f, 0.f, 0.f, 0.f}; }
  issue(0); issue(1);
  for (int kt = 0; kt < NS; ++kt) {
    if (kt + 1 < NS) asm volatile("s_waitcnt vmcnt(5)" ::: "memory"); else asm volatile("s_waitcnt vmcnt(0)" ::: "memory");
    __builtin_amdgcn_s_barrier();
    asm volatile("" ::: "memory");
    if (kt + 2 < NS) issue(kt + 2);
    const char* al = lds + (kt % 3) * 40960;
    const char* wl = al + 24576;
#pragma unroll
    for (int k2 = 0; k2 < 2; ++k2) {
      const int ks = 2 * kh + k2;
      bf16x8 af[3];
#pragma unroll
      for (int i = 0; i < 3; ++i) {
        const int row = wm * 48 + i * 16 + r16;
        af[i] = *(const bf16x8*)(al + row * 256 + (((4 * ks + g) ^ (row & 15)) * 16));
      }
#pragma unroll
      for (int nb = 0; nb < 2; ++nb) {
        const int row0 = 32 * ks + 8 * g + q4;
        const int col = wn * 32 + nb * 16 + 4 * p4;
        s16x4 b0 = tr_read(wl, row0 * 128 + ((((col >> 3)) ^ (row0 & 7)) * 16) + (col & 4) * 2);
        s16x4 b1 = tr_read(wl, (row0 + 4) * 128 + ((((col >> 3)) ^ ((row0 + 4) & 7)) * 16) + (col & 4) * 2);
        bf16x8 bf = join(b0, b1);
#pragma unroll
        for (int i = 0; i < 3; ++i) acc[i][nb] = __builtin_amdgcn_mfma_f32_16x16x32_bf16(af[i], bf, acc[i][nb], 0, 0, 0);
      }
    }
  }
  __syncthreads();
  float* red = (float*)lds;
  if (kh == 1) {
#pragma unroll
    for (int i = 0; i < 3; ++i)
#pragma unroll
      for (int nb = 0; nb < 2; ++nb) *(f32x4*)(red + (((w & 3) * 6 + i * 2 + nb) * 64 + lane) * 4) = acc[i][nb];
  }
  __syncthreads();
  if (kh == 0) {
#pragma unroll
    for (int i = 0; i < 3; ++i)
#pragma unroll
      for (int nb = 0; nb < 2; ++nb) {
        f32x4 o = acc[i][nb] + *(const f32x4*)(red + (((w & 3) * 6 + i * 2 + nb) * 64 + lane) * 4);
#pragma unroll
        for (int r = 0; r < 4; ++r) {
          const int m = tm * 96 + wm * 48 + i * 16 + 4 * g + r;
          const int n = tn * 64 + wn * 32 + nb * 16 + r16;
          out[(size_t)m * 1024 + blk * 512 + n] = (u16)(cvt_pk(o[r], 0.f) & 0xffff);
        }
      }
  }
}

extern "C" void kernel_launch(void* const* d_in, const int* in_sizes, int n_in,
                              void* d_out, int out_size, void* d_ws, size_t ws_size,
                              hipStream_t stream) {
  const u16* xA  = (const u16*)d_in[0];
  const u16* kA  = (const u16*)d_in[1];
  const u16* vA  = (const u16*)d_in[2];
  const u16* wqA = (const u16*)d_in[3];
  const u16* woA = (const u16*)d_in[4];
  const u16* xB  = (const u16*)d_in[5];
  const u16* kB  = (const u16*)d_in[6];
  const u16* vB  = (const u16*)d_in[7];
  const u16* wqB = (const u16*)d_in[8];
  const u16* woB = (const u16*)d_in[9];
  u16* ows = (u16*)d_ws;
  attn_kernel<<<512, 256, 0, stream>>>(xA, kA, vA, wqA, xB, kB, vB, wqB, ows);
  oproj_kernel<<<256, 512, 0, stream>>>(ows, woA, woB, (u16*)d_out);
}
```
